```python
import torch

def _softmax(x, axis=-1):
    return torch.softmax(x, dim=axis)
def _gelu(x):
    return torch.nn.functional.gelu(x, approximate="tanh")
def _silu(x):
    return torch.nn.functional.silu(x)
def _sigmoid(x):
    return torch.sigmoid(x)
def _layernorm(x, axis=-1, eps=1e-5):
    m = x.mean(dim=axis, keepdim=True)
    v = x.var(dim=axis, keepdim=True, unbiased=False)
    return (x - m) / torch.sqrt(v + eps)
def _scatter_add(tgt, idx, src, axis=0):
    return tgt.clone().index_add_(axis, idx, src)
def _segment_sum(src, seg, n):
    out = torch.zeros((n,) + src.shape[1:], dtype=src.dtype)
    return out.index_add_(0, seg, src)
def _pad(x, pw):
    flat = tuple(v for pair in reversed(pw) for v in pair)
    return torch.nn.functional.pad(x, flat)

kernel_name = 'concat_heads__softmax_full_mha__linear_kernel_hedgehog__prompt'

def setup_inputs():

    _w = (lambda a: (lambda t: t.to(torch.bfloat16) if t.is_floating_point() else t)(torch.as_tensor(a)))
    def _A_setup():
        B, S, D = 1, 2048, 1024
        return {
            "x": _w((lambda *s: torch.randn(*s, dtype=torch.bfloat16) * 0.2)(B, S, D)),
            "wqkv": _w((lambda *s: torch.randn(*s, dtype=torch.bfloat16) * 0.2)(D, 3 * D)),
            "wo": _w((lambda *s: torch.randn(*s, dtype=torch.bfloat16) * 0.05)(D, D)),
        }
    _A_in = _A_setup()

    def _B_setup():
        B, S, D, H = 1, 2048, 1024, 32
        Dh = D // H
        return {
            "x": _w((lambda *s: torch.randn(*s, dtype=torch.bfloat16) * 0.4)(B, S, D)),
            "wqkv": _w((lambda *s: torch.randn(*s, dtype=torch.bfloat16) * 0.4)(D, 3 * D)),
            "w_phi": _w((lambda *s: torch.randn(*s, dtype=torch.bfloat16) * 0.4)(H, Dh, Dh) * (Dh ** -0.5)),
            "wo": _w((lambda *s: torch.randn(*s, dtype=torch.bfloat16) * 0.05)(D, D)),
        }
    _B_in = _B_setup()

    return {
        "A_x": _A_in["x"],
        "A_wqkv": _A_in["wqkv"],
        "A_wo": _A_in["wo"],
        "B_x": _B_in["x"],
        "B_wqkv": _B_in["wqkv"],
        "B_w_phi": _B_in["w_phi"],
        "B_wo": _B_in["wo"],
    }

def reference(A_x, A_wqkv, A_wo, B_x, B_wqkv, B_w_phi, B_wo):
    def _A_ref(x, wqkv, wo):
        B, S, D = x.shape
        H = 32; Dh = D // H
        qkv = (lambda x, *d: x.permute(*d))((x @ wqkv).reshape(B, S, 3, H, Dh), 2, 0, 3, 1, 4)
        q, k, v = qkv[0], qkv[1], qkv[2]
        scale = Dh ** -0.5
        scores = torch.einsum("bhqd,bhkd->bhqk", q, k) * scale
        p = _softmax(scores, axis=-1)
        o = torch.einsum("bhqk,bhkd->bhqd", p, v)
        return (lambda x, *d: x.permute(*d))(o, 0, 2, 1, 3).reshape(B, S, D) @ wo

    def _B_ref(x, wqkv, w_phi, wo):
        B, S, D = x.shape
        H = 32; Dh = D // H
        qkv = (lambda x, *d: x.permute(*d))((x @ wqkv).reshape(B, S, 3, H, Dh), 2, 0, 3, 1, 4)
        q, k, v = qkv[0], qkv[1], qkv[2]
        pq = torch.einsum("bhsd,hdf->bhsf", q, w_phi)
        pk = torch.einsum("bhsd,hdf->bhsf", k, w_phi)
        phi_q = torch.concatenate([_softmax(pq, axis=-1), _softmax(-pq, axis=-1)], axis=-1)
        phi_k = torch.concatenate([_softmax(pk, axis=-1), _softmax(-pk, axis=-1)], axis=-1)
        kv = torch.einsum("bhsf,bhsd->bhfd", phi_k, v)
        z  = torch.einsum("bhsf->bhf", phi_k)
        num = torch.einsum("bhqf,bhfd->bhqd", phi_q, kv)
        den = torch.einsum("bhqf,bhf->bhq", phi_q, z)[..., None] + 1e-6
        o = num / den
        return (lambda x, *d: x.permute(*d))(o, 0, 2, 1, 3).reshape(B, S, D) @ wo

    a = _A_ref(A_x, A_wqkv, A_wo)
    b = _B_ref(B_x, B_wqkv, B_w_phi, B_wo)
    return torch.concatenate([a, b], axis=-1)
```

```cpp
#include <hip/hip_runtime.h>
#include <hip/hip_bf16.h>
#include <type_traits>

typedef short bf16x8 __attribute__((ext_vector_type(8)));
typedef short s16x4 __attribute__((ext_vector_type(4)));
typedef __bf16 bf16x8b __attribute__((ext_vector_type(8)));
typedef __bf16 bf16x16b __attribute__((ext_vector_type(16)));
typedef __bf16 bf16x2b __attribute__((ext_vector_type(2)));
typedef float f32x16 __attribute__((ext_vector_type(16)));
typedef float f32x4 __attribute__((ext_vector_type(4)));
typedef float f32x2 __attribute__((ext_vector_type(2)));
typedef unsigned u32x4 __attribute__((ext_vector_type(4)));
typedef unsigned u32x2 __attribute__((ext_vector_type(2)));
typedef unsigned u32x8 __attribute__((ext_vector_type(8)));

#define DI __device__ __forceinline__
#define LAS __attribute__((address_space(3)))
#define MFMA(a, b, c) __builtin_amdgcn_mfma_f32_32x32x16_bf16((a), (b), (c), 0, 0, 0)
#define WAIT_V(n) asm volatile("s_waitcnt vmcnt(" #n ")" ::: "memory")
#define WAIT_L(n) asm volatile("s_waitcnt lgkmcnt(" #n ")" ::: "memory")
#define BAR() __builtin_amdgcn_s_barrier()
#define SCHED() __builtin_amdgcn_sched_barrier(0)
#define SGB(m, n) __builtin_amdgcn_sched_group_barrier((m), (n), 0)

typedef LAS char* lptr;

DI void glds16(const char* g, unsigned voff, lptr l) {
  unsigned sv;
  asm volatile("s_mov_b32 %0, m0\n\ts_mov_b32 m0, %3\n\ts_nop 0\n\tglobal_load_lds_dwordx4 %1, %2\n\ts_mov_b32 m0, %0"
               : "=&s"(sv) : "v"(voff), "s"(g), "s"((unsigned)(uintptr_t)l) : "memory");
}
DI bf16x8 lds128(lptr p) { return *(LAS bf16x8*)p; }
DI s16x4 ldstr(lptr p) { return __builtin_amdgcn_ds_read_tr16_b64_v4i16((LAS s16x4*)p); }
DI bf16x8 cat(s16x4 a, s16x4 b) { return __builtin_shufflevector(a, b, 0, 1, 2, 3, 4, 5, 6, 7); }
DI unsigned cvtpk(float lo, float hi) {
  f32x2 v = {lo, hi};
  return __builtin_bit_cast(unsigned, __builtin_convertvector(v, bf16x2b));
}
DI bf16x8 pack8(const f32x16& x, int s) {
  u32x4 p;
#pragma unroll
  for (int i = 0; i < 4; ++i) p[i] = cvtpk(x[8 * s + 2 * i], x[8 * s + 2 * i + 1]);
  return __builtin_bit_cast(bf16x8, p);
}
DI float xhalf_max(float v) {
  u32x2 r = __builtin_amdgcn_permlane32_swap(__float_as_uint(v), __float_as_uint(v), false, false);
  return fmaxf(__uint_as_float(r[0]), __uint_as_float(r[1]));
}
DI float xhalf_min(float v) {
  u32x2 r = __builtin_amdgcn_permlane32_swap(__float_as_uint(v), __float_as_uint(v), false, false);
  return fminf(__uint_as_float(r[0]), __uint_as_float(r[1]));
}
DI float xhalf_add(float v) {
  u32x2 r = __builtin_amdgcn_permlane32_swap(__float_as_uint(v), __float_as_uint(v), false, false);
  return __uint_as_float(r[0]) + __uint_as_float(r[1]);
}
DI f32x16 zero16() {
  f32x16 z;
#pragma unroll
  for (int i = 0; i < 16; ++i) z[i] = 0.f;
  return z;
}

constexpr float LOG2E = 1.4426950408889634f;
constexpr float QSCALE = 0.17677669529663687f * LOG2E;

constexpr size_t WS_QA = 0;
constexpr size_t WS_KVA = 4u << 20;
constexpr size_t WS_QB = 12u << 20;
constexpr size_t WS_O = 16u << 20;
constexpr size_t WS_KVP = 24u << 20;
constexpr int KVP_STRIDE = 2112;

DI void dual_softmax(const f32x16& p, f32x16& ep, f32x16& em) {
  float mx = p[0], mn = p[0];
#pragma unroll
  for (int i = 1; i < 16; ++i) { mx = fmaxf(mx, p[i]); mn = fminf(mn, p[i]); }
  mx = xhalf_max(mx);
  mn = xhalf_min(mn);
  const float cx = -mx * LOG2E, cn = mn * LOG2E;
  float sp = 0.f, sm = 0.f;
#pragma unroll
  for (int i = 0; i < 16; ++i) {
    ep[i] = __builtin_amdgcn_exp2f(__builtin_fmaf(p[i], LOG2E, cx));
    em[i] = __builtin_amdgcn_exp2f(__builtin_fmaf(p[i], -LOG2E, cn));
    sp += ep[i];
    sm += em[i];
  }
  sp = xhalf_add(sp);
  sm = xhalf_add(sm);
  const float rp = __builtin_amdgcn_rcpf(sp), rm = __builtin_amdgcn_rcpf(sm);
#pragma unroll
  for (int i = 0; i < 16; ++i) { ep[i] *= rp; em[i] *= rm; }
}

constexpr int K1_XOFF = 0, K1_WOFF = 98304, K1_POFF = 159744, K1_LDS = 163840;

struct Frags { bf16x8 x0, x1, wq, wk, wv; };

__global__ __launch_bounds__(512, 2) void k1_qkv(const char* __restrict__ xA, const char* __restrict__ wA,
                                                 const char* __restrict__ xB, const char* __restrict__ wB,
                                                 const char* __restrict__ wphi, char* __restrict__ ws) {
  __shared__ __attribute__((aligned(128))) char smem[K1_LDS];
  lptr lds = (lptr)smem;
  const int tid = threadIdx.x, lane = tid & 63;
  const int w = __builtin_amdgcn_readfirstlane(tid >> 6);
  const int bid = blockIdx.x, xcd = bid & 7, idx = bid >> 3;
  const int br = xcd >> 2, mt = ((xcd >> 1) & 1) * 4 + (idx & 3), hp = (xcd & 1) * 8 + (idx >> 2);
  const char* X = (br ? xB : xA) + (size_t)mt * 256 * 2048;
  const char* W = (br ? wB : wA) + hp * 128;
  const int wm = w & 3, wn = w >> 2;

  const unsigned voffX = (unsigned)((lane >> 3) * 2048 + (((lane & 7) ^ (((w & 1) << 2) | (lane >> 4))) << 4));
  const unsigned voffW = (unsigned)((lane >> 3) * 6144 + (((lane & 7) ^ (((lane >> 4) & 1) << 2)) << 4));
  const bool lowv = w < 4;
  const unsigned voffM = lowv ? voffW : voffX;
  const char* Xw = X + (size_t)w * 16384;
  const char* Ww = W + (size_t)(w & 3) * 8 * 6144 + (w >> 2) * 2048;
  const char* Mw = lowv ? Ww + 4096 : Xw;
  auto ldX = [&](int t, int slot, int piece_minus_w) {
    glds16(Xw + (size_t)(t * 128 + piece_minus_w * 16384), voffX, lds + K1_XOFF + slot * 32768 + (w + piece_minus_w) * 1024);
  };
  auto ldW = [&](int u, int slot) {
    glds16(Ww + (size_t)u * (32 * 6144), voffW, lds + K1_WOFF + slot * 12288 + w * 1024);
  };
  auto ldM = [&](int u, int wslot, int t, int xslot, int xp) {
    const unsigned go = lowv ? (unsigned)(u * (32 * 6144)) : (unsigned)(t * 128 + (xp - 4) * 16384);
    const int l = lowv ? (K1_WOFF + wslot * 12288 + 8 * 1024) : (K1_XOFF + xslot * 32768 + (xp - 4) * 1024);
    glds16(Mw + go, voffM, lds + l + w * 1024);
  };

  glds16(wphi + hp * 4096 + (w & 3) * 1024, lane * 16, lds + K1_POFF + (w & 3) * 1024);
#pragma unroll
  for (int t = 0; t < 2; ++t) {
    ldX(t, t, 0); ldX(t, t, 8); ldX(t, t, 16); ldX(t, t, 24);
    ldW(2 * t, 2 * t); ldW(2 * t + 1, 2 * t + 1);
    glds16(W + (size_t)((2 * t + (w >> 2)) * 32 + (w & 3) * 8) * 6144 + 4096, voffW,
           lds + K1_WOFF + (2 * t + (w >> 2)) * 12288 + (8 + (w & 3)) * 1024);
  }
  ldW(4, 4); ldM(4, 4, 2, 2, 16); ldX(2, 2, 0); ldX(2, 2, 8);

  const int r = lane & 31, h = lane >> 5;
  const int q4 = (lane >> 2) & 3, p4 = lane & 3, blk = (lane >> 4) & 1;
  const unsigned xbase = (unsigned)(K1_XOFF + (wm * 64 + r) * 128 + ((h ^ ((r >> 1) & 7)) << 4));
  const unsigned wbase = (unsigned)(K1_WOFF + (8 * h + q4) * 128 + ((wn ^ (q4 >> 1)) << 6) + blk * 32 + p4 * 8);
  auto rd = [&](Frags& f, int xslot, int s, int wslot, int sw) {
    lptr xa = lds + ((xbase + xslot * 32768) ^ (unsigned)(s << 5));
    lptr wa = lds + (wbase + wslot * 12288) + sw * 2048;
    f.x0 = lds128(xa);
    f.wq = cat(ldstr(wa), ldstr(wa + 512));
    f.x1 = lds128(xa + 4096);
    f.wk = cat(ldstr(wa + 4096), ldstr(wa + 4096 + 512));
    f.wv = cat(ldstr(wa + 8192), ldstr(wa + 8192 + 512));
  };
  f32x16 aq0 = zero16(), aq1 = zero16(), ak0 = zero16(), ak1 = zero16(), av0 = zero16(), av1 = zero16();
  auto phase = [&](const Frags& c, Frags& n, auto RD, int xslot, int s, int wslot, int sw, auto g0, auto g1, auto g2, auto g3) {
    lptr xa = lds + ((xbase + xslot * 32768) ^ (unsigned)(s << 5));
    lptr wa = lds + (wbase + wslot * 12288) + sw * 2048;
    SCHED();
    aq0 = MFMA(c.wq, c.x0, aq0);
    SCHED();
    if constexpr (decltype(RD)::value) { n.x0 = lds128(xa); n.wq = cat(ldstr(wa), ldstr(wa + 512)); }
    g0();
    SCHED();
    aq1 = MFMA(c.wq, c.x1, aq1);
    SCHED();
    if constexpr (decltype(RD)::value) { n.x1 = lds128(xa + 4096); n.wk = cat(ldstr(wa + 4096), ldstr(wa + 4096 + 512)); }
    g1();
    SCHED();
    ak0 = MFMA(c.wk, c.x0, ak0);
    SCHED();
    if constexpr (decltype(RD)::value) { n.wv = cat(ldstr(wa + 8192), ldstr(wa + 8192 + 512)); }
    g2();
    SCHED();
    ak1 = MFMA(c.wk, c.x1, ak1);
    SCHED();
    g3();
    SCHED();
    av0 = MFMA(c.x0, c.wv, av0);
    av1 = MFMA(c.x1, c.wv, av1);
    SCHED();
  };
  auto nop = [] {};
  using T = std::integral_constant<bool, true>;
  using F = std::integral_constant<bool, false>;

  Frags f0, f1;
  WAIT_V(11);
  BAR();
  rd(f0, 0, 0, 0, 0);
  int xs = 0, ws0 = 0;
  auto stage = [&](int t, auto LE, auto LO, auto WV, auto LAST) {
    const int xs1 = xs == 2 ? 0 : xs + 1, xsp = xs == 0 ? 2 : xs - 1;
    const int ws1 = ws0 == 4 ? 0 : ws0 + 1, ws2 = ws1 == 4 ? 0 : ws1 + 1;
    phase(f0, f1, T{}, xs, 1, ws0, 1, nop, nop, nop, nop);
    WAIT_L(0);
    BAR();
    if constexpr (decltype(LE)::value)
      phase(f1, f0, T{}, xs, 2, ws1, 0, [&] { ldX(t + 2, xsp, 24); }, [&] { ldM(2 * t + 5, ws0, t + 2, xsp, 20); },
            [&] { ldW(2 * t + 5, ws0); }, nop);
    else
      phase(f1, f0, T{}, xs, 2, ws1, 0, nop, nop, nop, nop);
    phase(f0, f1, T{}, xs, 3, ws1, 1, nop, nop, nop, nop);
    WAIT_L(0);
    if constexpr (decltype(WV)::value == 7) WAIT_V(7);
    if constexpr (decltype(WV)::value == 0) WAIT_V(0);
    BAR();
    if constexpr (decltype(LO)::value)
      phase(f1, f0, T{}, xs1, 0, ws2, 0, [&] { ldW(2 * t + 6, ws1); }, [&] { ldM(2 * t + 6, ws1, t + 3, xs, 16); },
            [&] { ldX(t + 3, xs, 0); }, [&] { ldX(t + 3, xs, 8); });
    else
      phase(f1, f0, std::integral_constant<bool, !decltype(LAST)::value>{}, xs1, 0, ws2, 0, nop, nop, nop, nop);
    xs = xs1;
    ws0 = ws2;
  };
  using I7 = std::integral_constant<int, 7>;
  using I0 = std::integral_constant<int, 0>;
  using IN = std::integral_constant<int, -1>;
#pragma unroll 1
  for (int t = 0; t < 13; ++t) stage(t, T{}, T{}, I7{}, F{});
  stage(13, T{}, F{}, I7{}, F{});
  stage(14, F{}, F{}, I0{}, F{});
  stage(15, F{}, F{}, IN{}, T{});

  const int head = hp * 2 + wn;
  const int tb = mt * 8 + wm * 2;
  if (br == 0) {
#pragma unroll
    for (int i = 0; i < 16; ++i) { aq0[i] *= QSCALE; aq1[i] *= QSCALE; }
    char* qd = ws + WS_QA + ((size_t)(head * 64 + tb) * 2048) + lane * 16;
    *(bf16x8*)(qd) = pack8(aq0, 0);
    *(bf16x8*)(qd + 1024) = pack8(aq0, 1);
    *(bf16x8*)(qd + 2048) = pack8(aq1, 0);
    *(bf16x8*)(qd + 3072) = pack8(aq1, 1);
    char* kd = ws + WS_KVA + ((size_t)(head * 64 + tb) * 4096) + lane * 16;
    *(bf16x8*)(kd) = pack8(ak0, 0);
    *(bf16x8*)(kd + 1024) = pack8(ak0, 1);
    *(bf16x8*)(kd + 2048) = pack8(av0, 0);
    *(bf16x8*)(kd + 3072) = pack8(av0, 1);
    *(bf16x8*)(kd + 4096) = pack8(ak1, 0);
    *(bf16x8*)(kd + 5120) = pack8(ak1, 1);
    *(bf16x8*)(kd + 6144) = pack8(av1, 0);
    *(bf16x8*)(kd + 7168) = pack8(av1, 1);
    return;
  }
  {
    char* qd = ws + WS_QB + ((size_t)(head * 64 + tb) * 2048) + lane * 16;
    *(bf16x8*)(qd) = pack8(aq0, 0);
    *(bf16x8*)(qd + 1024) = pack8(aq0, 1);
    *(bf16x8*)(qd + 2048) = pack8(aq1, 0);
    *(bf16x8*)(qd + 3072) = pack8(aq1, 1);
  }
  bf16x8 wf[2];
  {
    lptr pa = lds + K1_POFF + wn * 2048 + (4 * h + q4) * 64 + blk * 32 + p4 * 8;
    wf[0] = cat(ldstr(pa), ldstr(pa + 512));
    wf[1] = cat(ldstr(pa + 1024), ldstr(pa + 1536));
  }
#pragma unroll
  for (int i = 0; i < 2; ++i) {
    const f32x16& ak = i ? ak1 : ak0;
    const f32x16& av = i ? av1 : av0;
    f32x16 p = MFMA(wf[0], pack8(ak, 0), zero16());
    p = MFMA(wf[1], pack8(ak, 1), p);
    f32x16 ep, em;
    dual_softmax(p, ep, em);
    const int tok = wm * 64 + i * 32 + r;
    lptr pw = lds + wn * 32768 + tok * 64;
    const int sw = (r >> 1) & 7;
#pragma unroll
    for (int g = 0; g < 4; ++g) {
      u32x2 a = {cvtpk(ep[4 * g], ep[4 * g + 1]), cvtpk(ep[4 * g + 2], ep[4 * g + 3])};
      u32x2 b = {cvtpk(em[4 * g], em[4 * g + 1]), cvtpk(em[4 * g + 2], em[4 * g + 3])};
      *(LAS u32x2*)(pw + (((2 * g + h) ^ sw) << 3)) = a;
      *(LAS u32x2*)(pw + 16384 + (((2 * g + h) ^ sw) << 3)) = b;
    }
    lptr vw = lds + 65536 + wn * 16384 + (wm * 2 + i) * 2048 + lane * 16;
    *(LAS bf16x8*)(vw) = pack8(av, 0);
    *(LAS bf16x8*)(vw + 1024) = pack8(av, 1);
  }
  WAIT_L(0);
  BAR();
  {
    const int hd = w & 1, sg = (w >> 1) & 1, zsel = w >> 2;
    bf16x8 ones;
#pragma unroll
    for (int j = 0; j < 8; ++j) ones[j] = 0x3f80;
    f32x16 y = zero16();
    lptr pb = lds + hd * 32768 + sg * 16384;
    lptr vb = lds + 65536 + hd * 16384 + lane * 16;
#pragma unroll
    for (int ks = 0; ks < 16; ++ks) {
      const int t0 = 16 * ks + 4 * h + q4, t1 = t0 + 8;
      s16x4 lo = ldstr(pb + t0 * 64 + (((4 * blk + p4) ^ ((t0 >> 1) & 7)) << 3));
      s16x4 hi = ldstr(pb + t1 * 64 + (((4 * blk + p4) ^ ((t1 >> 1) & 7)) << 3));
      bf16x8 bv = zsel ? ones : lds128(vb + ks * 1024);
      y = MFMA(cat(lo, hi), bv, y);
    }
    float* dst = (float*)(ws + WS_KVP) + (size_t)((hp * 2 + hd) * 8 + mt) * KVP_STRIDE;
    if (!zsel) {
#pragma unroll
      for (int s = 0; s < 2; ++s) {
        float* d = dst + sg * 1024 + s * 512 + lane * 8;
        *(f32x4*)(d) = f32x4{y[8 * s], y[8 * s + 1], y[8 * s + 2], y[8 * s + 3]};
        *(f32x4*)(d + 4) = f32x4{y[8 * s + 4], y[8 * s + 5], y[8 * s + 6], y[8 * s + 7]};
      }
    } else if (r == 0) {
#pragma unroll
      for (int s = 0; s < 2; ++s) {
        float* d = dst + 2048 + sg * 32 + s * 16 + h * 8;
        *(f32x4*)(d) = f32x4{y[8 * s], y[8 * s + 1], y[8 * s + 2], y[8 * s + 3]};
        *(f32x4*)(d + 4) = f32x4{y[8 * s + 4], y[8 * s + 5], y[8 * s + 6], y[8 * s + 7]};
      }
    }
  }
}

constexpr int K2_KVF = 131072, K2_ZF = K2_KVF + 4096, K2_PH = K2_ZF + 128, K2_LDS = K2_PH + 2048;

__global__ __launch_bounds__(1024, 4) void k2_attn(const char* __restrict__ wphi, char* __restrict__ ws) {
  __shared__ __attribute__((aligned(128))) char smem[K2_LDS];
  lptr lds = (lptr)smem;
  const int tid = threadIdx.x, lane = tid & 63;
  const int w = __builtin_amdgcn_readfirstlane(tid >> 6);
  const int bid = blockIdx.x, xcd = bid & 7, idx = bid >> 3;
  const int head = xcd * 4 + (idx >> 3), qb = idx & 7;
  const int qt = w & 7, kh = w >> 3;
  const int tb = qb * 8 + qt;
  const int r = lane & 31, h = lane >> 5;
  const int q4 = (lane >> 2) & 3, p4 = lane & 3, blk = (lane >> 4) & 1;
  const char* kvg = ws + WS_KVA + (size_t)(head * 64 + kh * 32) * 4096;
  const unsigned voff = (unsigned)(lane * 16);
  auto ldc = [&](int c, int slot) {
    const char* g = kvg + (size_t)(c * 16384 + qt * 2048);
    lptr l = lds + slot * 32768 + kh * 16384 + qt * 2048;
    glds16(g, voff, l);
    glds16(g + 1024, voff, l + 1024);
  };

  ldc(0, 0);
  if (w < 2) glds16(wphi + head * 2048 + w * 1024, voff, lds + K2_PH + w * 1024);
  const char* qg = ws + WS_QA + (size_t)(head * 64 + tb) * 2048 + lane * 16;
  const bf16x8 qa0 = *(const bf16x8*)qg, qa1 = *(const bf16x8*)(qg + 1024);
  const char* qbg = ws + WS_QB + (size_t)(head * 64 + tb) * 2048 + lane * 16;
  bf16x8 qb0 = qa0, qb1 = qa1;
  if (kh) { qb0 = *(const bf16x8*)qbg; qb1 = *(const bf16x8*)(qbg + 1024); }
  {
    const float* pp = (const float*)(ws + WS_KVP) + (size_t)head * 8 * KVP_STRIDE + tid * 2;
    f32x2 a = {0.f, 0.f}, z = {0.f, 0.f};
#pragma unroll
    for (int m = 0; m < 8; ++m) a += *(const f32x2*)(pp + m * KVP_STRIDE);
    if (tid < 32) {
#pragma unroll
      for (int m = 0; m < 8; ++m) z += *(const f32x2*)(pp + 2048 + m * KVP_STRIDE);
      *(LAS unsigned*)(lds + K2_ZF + tid * 4) = cvtpk(z[0], z[1]);
    }
    *(LAS unsigned*)(lds + K2_KVF + tid * 4) = cvtpk(a[0], a[1]);
  }
  WAIT_V(0);
  SCHED();
  ldc(1, 1); ldc(2, 2); ldc(3, 3);
  WAIT_L(0);
  BAR();
  SCHED();

  if (kh) {
    lptr pa = lds + K2_PH + (4 * h + q4) * 64 + blk * 32 + p4 * 8;
    bf16x8 wf0 = cat(ldstr(pa), ldstr(pa + 512)), wf1 = cat(ldstr(pa + 1024), ldstr(pa + 1536));
    f32x16 p = MFMA(wf0, qb0, zero16());
    p = MFMA(wf1, qb1, p);
    f32x16 ep, em;
    dual_softmax(p, ep, em);
    bf16x8 ph[4] = {pack8(ep, 0), pack8(ep, 1), pack8(em, 0), pack8(em, 1)};
    f32x16 num = zero16(), den = zero16();
#pragma unroll
    for (int i = 0; i < 4; ++i) {
      num = MFMA(lds128(lds + K2_KVF + i * 1024 + lane * 16), ph[i], num);
      den = MFMA(lds128(lds + K2_ZF + i * 32 + h * 16), ph[i], den);
    }
    const float rd = 1.0f / (den[0] + 1e-6f);
#pragma unroll
    for (int i = 0; i < 16; ++i) num[i] *= rd;
    char* od = ws + WS_O + (4u << 20) + (size_t)(tb * 32 + head) * 2048 + lane * 16;
    *(bf16x8*)(od) = pack8(num, 0);
    *(bf16x8*)(od + 1024) = pack8(num, 1);
  }

  lptr kb = lds + kh * 16384 + lane * 16;
  bf16x8 k0 = lds128(kb), k1 = lds128(kb + 1024);
  float mref;
  {
    f32x16 s = MFMA(k0, qa0, zero16());
    s = MFMA(k1, qa1, s);
    float m = s[0];
#pragma unroll
    for (int i = 1; i < 16; ++i) m = fmaxf(m, s[i]);
    mref = xhalf_max(m);
  }
  f32x16 negm;
#pragma unroll
  for (int i = 0; i < 16; ++i) negm[i] = -mref;
  bf16x8b sel;
  {
    const bool on = (lane & 3) < 2;
    const bool odd = (lane >> 2) & 1;
#pragma unroll
    for (int j = 0; j < 8; ++j) sel[j] = (on && ((j >= 4) == odd)) ? (__bf16)1.0f : (__bf16)0.0f;
  }
  const int sidx = (lane & 1) ? 0xEEEE : 0x4444;
  f32x16 o = zero16();
  f32x4 l4 = {0.f, 0.f, 0.f, 0.f};
  auto tile = [&](lptr cur, lptr nxt) {
    f32x16 s = MFMA(k0, qa0, negm);
    s = MFMA(k1, qa1, s);
    const bf16x8 v0 = lds128(cur + 2048), v1 = lds128(cur + 3072);
    k0 = lds128(nxt);
    k1 = lds128(nxt + 1024);
#pragma unroll
    for (int i = 0; i < 16; ++i) s[i] = __builtin_amdgcn_exp2f(s[i]);
    u32x8 pp;
#pragma unroll
    for (int i = 0; i < 8; ++i) pp[i] = cvtpk(s[2 * i], s[2 * i + 1]);
    const bf16x8 pa = __builtin_bit_cast(bf16x8, u32x4{pp[0], pp[1], pp[2], pp[3]});
    const bf16x8 pb = __builtin_bit_cast(bf16x8, u32x4{pp[4], pp[5], pp[6], pp[7]});
    o = MFMA(v0, pa, o);
    o = MFMA(v1, pb, o);
    l4 = __builtin_amdgcn_smfmac_f32_16x16x64_bf16(sel, __builtin_bit_cast(bf16x16b, pp), l4, sidx, 0, 0);
  };
#pragma unroll 1
  for (int c = 0; c < 8; ++c) {
    lptr cb = kb + (c & 3) * 32768;
    lptr nb = kb + ((c + 1) & 3) * 32768;
    tile(cb, cb + 4096);
    tile(cb + 4096, cb + 8192);
    tile(cb + 8192, cb + 12288);
    if (c < 5) { WAIT_V(2); } else { WAIT_V(0); }
    BAR();
    if (c >= 1 && c < 5) ldc(c + 3, (c + 3) & 3);
    tile(cb + 12288, nb);
  }
  float l = l4[0] + l4[1];
  float m = mref;
  {
    float a = fabsf(l);
#pragma unroll
    for (int i = 0; i < 16; ++i) a = fmaxf(a, fabsf(o[i]));
    bool bad = !(a < 1e30f);
#pragma unroll
    for (int i = 0; i < 16; ++i) bad = bad || (o[i] != o[i]);
    bad = bad || (l != l);
    if (__builtin_amdgcn_ballot_w64(bad) != 0) {
      o = zero16();
      m = -INFINITY;
      float ls = 0.f;
#pragma unroll 1
      for (int t = 0; t < 32; ++t) {
        const char* g = kvg + (size_t)t * 4096 + lane * 16;
        f32x16 s = MFMA(*(const bf16x8*)g, qa0, zero16());
        s = MFMA(*(const bf16x8*)(g + 1024), qa1, s);
        float tm = s[0];
#pragma unroll
        for (int i = 1; i < 16; ++i) tm = fmaxf(tm, s[i]);
        const float mn = fmaxf(m, xhalf_max(tm));
        const float al = __builtin_amdgcn_exp2f(m - mn);
        m = mn;
        ls *= al;
#pragma unroll
        for (int i = 0; i < 16; ++i) { o[i] *= al; s[i] = __builtin_amdgcn_exp2f(s[i] - mn); ls += s[i]; }
        o = MFMA(*(const bf16x8*)(g + 2048), pack8(s, 0), o);
        o = MFMA(*(const bf16x8*)(g + 3072), pack8(s, 1), o);
      }
      l = xhalf_add(ls);
    }
  }
  lptr cbuf = lds + qt * 5120 + lane * 16;
  if (kh) {
#pragma unroll
    for (int g = 0; g < 4; ++g) *(LAS f32x4*)(cbuf + g * 1024) = f32x4{o[4 * g], o[4 * g + 1], o[4 * g + 2], o[4 * g + 3]};
    *(LAS f32x4*)(cbuf + 4096) = f32x4{m, l, 0.f, 0.f};
  }
  WAIT_L(0);
  BAR();
  if (!kh) {
    const f32x4 ml = *(LAS f32x4*)(cbuf + 4096);
    const float mm = fmaxf(m, ml[0]);
    const float a0 = __builtin_amdgcn_exp2f(m - mm), a1 = __builtin_amdgcn_exp2f(ml[0] - mm);
    const float rl = 1.0f / (l * a0 + ml[1] * a1);
    const float c0 = a0 * rl, c1 = a1 * rl;
#pragma unroll
    for (int g = 0; g < 4; ++g) {
      const f32x4 t = *(LAS f32x4*)(cbuf + g * 1024);
#pragma unroll
      for (int j = 0; j < 4; ++j) o[4 * g + j] = o[4 * g + j] * c0 + t[j] * c1;
    }
    char* od = ws + WS_O + (size_t)(tb * 32 + head) * 2048 + lane * 16;
    *(bf16x8*)(od) = pack8(o, 0);
    *(bf16x8*)(od + 1024) = pack8(o, 1);
  }
}

constexpr int K3_LDS = 163840;
struct Frags3 { bf16x8 o0, o1, w0, w1; };

__global__ __launch_bounds__(512, 2) void k3_proj(const char* __restrict__ woA, const char* __restrict__ woB,
                                                  const char* __restrict__ ws, char* __restrict__ out) {
  __shared__ __attribute__((aligned(128))) char smem[K3_LDS];
  lptr lds = (lptr)smem;
  const int tid = threadIdx.x, lane = tid & 63;
  const int w = __builtin_amdgcn_readfirstlane(tid >> 6);
  const int bid = blockIdx.x, xcd = bid & 7, idx = bid >> 3;
  const int br = xcd >> 2, mt = (xcd & 3) * 4 + (idx & 3), nt = idx >> 2;
  const int wm = w & 1, wn = (w >> 1) & 1, kh = w >> 2;
  const int r = lane & 31, h = lane >> 5;
  const int q4 = (lane >> 2) & 3, p4 = lane & 3, blk = (lane >> 4) & 1;
  const char* og = ws + WS_O + (size_t)br * (4u << 20) + (size_t)(mt * 4 + (w >> 1)) * 65536 + (w & 1) * 2048;
  const char* wg = (br ? woB : woA) + nt * 256 + (size_t)w * 8 * 2048;
  const unsigned voffO = (unsigned)(lane * 16);
  const unsigned voffW = (unsigned)((lane >> 4) * 2048 + (((lane & 15) ^ ((lane >> 4) << 2)) << 4));
  auto ld = [&](int t, int slot) {
    lptr l = lds + slot * 32768 + w * 2048;
    glds16(og + t * 4096, voffO, l);
    glds16(og + t * 4096 + 1024, voffO, l + 1024);
    glds16(wg + (size_t)t * (64 * 2048), voffW, l + 16384);
    glds16(wg + (size_t)t * (64 * 2048) + 4 * 2048, voffW, l + 16384 + 1024);
  };
#pragma unroll
  for (int t = 0; t < 5; ++t) ld(t, t);
  const unsigned obase = (unsigned)(wm * 8192 + kh * 2048 + lane * 16);
  const unsigned wbase = (unsigned)(16384 + (kh * 32 + 4 * h + q4) * 256 + (((wn * 2) ^ q4) << 6) + blk * 32 + p4 * 8);
  auto rd = [&](Frags3& f, int slot, int s) {
    lptr oa = lds + (obase + slot * 32768) + s * 1024;
    lptr wa = lds + (wbase + slot * 32768) + s * 4096;
    lptr wb = lds + ((wbase + slot * 32768) ^ 64u) + s * 4096;
    f.o0 = lds128(oa);
    f.w0 = cat(ldstr(wa), ldstr(wa + 2048));
    f.o1 = lds128(oa + 4096);
    f.w1 = cat(ldstr(wb), ldstr(wb + 2048));
  };
  f32x16 a00 = zero16(), a01 = zero16(), a10 = zero16(), a11 = zero16();
  auto phase = [&](const Frags3& c, Frags3& n, auto RD, int slot, int s, auto LD, int t, int lslot) {
    lptr oa = lds + (obase + slot * 32768) + s * 1024;
    lptr wa = lds + (wbase + slot * 32768) + s * 4096;
    lptr wb = lds + ((wbase + slot * 32768) ^ 64u) + s * 4096;
    lptr l = lds + lslot * 32768 + w * 2048;
    SCHED();
    a00 = MFMA(c.w0, c.o0, a00);
    SCHED();
    if constexpr (decltype(RD)::value) { n.o0 = lds128(oa); n.w0 = cat(ldstr(wa), ldstr(wa + 2048)); }
    if constexpr (decltype(LD)::value) glds16(og + t * 4096, voffO, l);
    SCHED();
    a10 = MFMA(c.w0, c.o1, a10);
    SCHED();
    if constexpr (decltype(RD)::value) { n.o1 = lds128(oa + 4096); n.w1 = cat(ldstr(wb), ldstr(wb + 2048)); }
    if constexpr (decltype(LD)::value) glds16(og + t * 4096 + 1024, voffO, l + 1024);
    SCHED();
    a01 = MFMA(c.w1, c.o0, a01);
    SCHED();
    if constexpr (decltype(LD)::value) {
      glds16(wg + (size_t)t * (64 * 2048), voffW, l + 16384);
      glds16(wg + (size_t)t * (64 * 2048) + 4 * 2048, voffW, l + 16384 + 1024);
    }
    SCHED();
    a11 = MFMA(c.w1, c.o1, a11);
    SCHED();
  };
  using T = std::integral_constant<bool, true>;
  using F = std::integral_constant<bool, false>;
  Frags3 f0, f1;
  WAIT_V(16);
  BAR();
  rd(f0, 0, 0);
  int sl = 0;
  auto stage = [&](int t, auto LD, auto WV, auto LAST) {
    const int sl1 = sl == 4 ? 0 : sl + 1;
    phase(f0, f1, T{}, sl, 1, F{}, 0, 0);
    WAIT_L(0);
    if constexpr (decltype(WV)::value == 12) WAIT_V(12);
    if constexpr (decltype(WV)::value == 8) WAIT_V(8);
    if constexpr (decltype(WV)::value == 4) WAIT_V(4);
    if constexpr (decltype(WV)::value == 0) WAIT_V(0);
    BAR();
    phase(f1, f0, std::integral_constant<bool, !decltype(LAST)::value>{}, sl1, 0, LD, t + 5, sl);
    sl = sl1;
  };
#pragma unroll 1
  for (int t = 0; t < 11; ++t) stage(t, T{}, std::integral_constant<int, 12>{}, F{});
  stage(11, F{}, std::integral_constant<int, 12>{}, F{});
  stage(12, F{}, std::integral_constant<int, 8>{}, F{});
  stage(13, F{}, std::integral_constant<int, 4>{}, F{});
  stage(14, F{}, std::integral_constant<int, 0>{}, F{});
  stage(15, F{}, std::integral_constant<int, -1>{}, T{});

  {
    lptr xw = lds + w * 8192 + lane * 16;
    lptr xr = lds + (w ^ 4) * 8192 + lane * 16;
    f32x16 s0 = kh ? a00 : a10, s1 = kh ? a01 : a11;
    f32x16 c0 = kh ? a10 : a00, c1 = kh ? a11 : a01;
#pragma unroll
    for (int g = 0; g < 4; ++g) {
      *(LAS f32x4*)(xw + g * 1024) = f32x4{s0[4 * g], s0[4 * g + 1], s0[4 * g + 2], s0[4 * g + 3]};
      *(LAS f32x4*)(xw + 4096 + g * 1024) = f32x4{s1[4 * g], s1[4 * g + 1], s1[4 * g + 2], s1[4 * g + 3]};
    }
    WAIT_L(0);
    BAR();
#pragma unroll
    for (int g = 0; g < 4; ++g) {
      const f32x4 t0 = *(LAS f32x4*)(xr + g * 1024), t1 = *(LAS f32x4*)(xr + 4096 + g * 1024);
#pragma unroll
      for (int j = 0; j < 4; ++j) { c0[4 * g + j] += t0[j]; c1[4 * g + j] += t1[j]; }
    }
    const int row = mt * 128 + wm * 64 + kh * 32 + r;
    char* od = out + (size_t)row * 4096 + br * 2048 + nt * 256 + wn * 128 + h * 16;
#pragma unroll
    for (int j = 0; j < 2; ++j) {
      const f32x16& c = j ? c1 : c0;
#pragma unroll
      for (int m = 0; m < 2; ++m) {
        const unsigned pa0 = cvtpk(c[8 * m], c[8 * m + 1]), pa1 = cvtpk(c[8 * m + 2], c[8 * m + 3]);
        const unsigned pb0 = cvtpk(c[8 * m + 4], c[8 * m + 5]), pb1 = cvtpk(c[8 * m + 6], c[8 * m + 7]);
        const u32x2 x0 = __builtin_amdgcn_permlane32_swap(pa0, pb0, false, false);
        const u32x2 x1 = __builtin_amdgcn_permlane32_swap(pa1, pb1, false, false);
        *(u32x4*)(od + j * 64 + m * 32) = u32x4{x0[0], x1[0], x0[1], x1[1]};
      }
    }
  }
}

extern "C" void kernel_launch(void* const* d_in, const int* in_sizes, int n_in, void* d_out, int out_size, void* d_ws,
                              size_t ws_size, hipStream_t stream) {
  const char* xA = (const char*)d_in[0];
  const char* wA = (const char*)d_in[1];
  const char* woA = (const char*)d_in[2];
  const char* xB = (const char*)d_in[3];
  const char* wB = (const char*)d_in[4];
  const char* wphi = (const char*)d_in[5];
  const char* woB = (const char*)d_in[6];
  char* ws = (char*)d_ws;
  k1_qkv<<<256, 512, 0, stream>>>(xA, wA, xB, wB, wphi, ws);
  k2_attn<<<256, 1024, 0, stream>>>(wphi, ws);
  k3_proj<<<256, 512, 0, stream>>>(woA, woB, ws, (char*)d_out);
}
```

```cpp
#include <hip/hip_runtime.h>
#include <type_traits>

typedef short bf16x8 __attribute__((ext_vector_type(8)));
typedef short s16x4 __attribute__((ext_vector_type(4)));
typedef __bf16 bf16x8b __attribute__((ext_vector_type(8)));
typedef __bf16 bf16x16b __attribute__((ext_vector_type(16)));
typedef __bf16 bf16x2b __attribute__((ext_vector_type(2)));
typedef float f32x16 __attribute__((ext_vector_type(16)));
typedef float f32x4 __attribute__((ext_vector_type(4)));
typedef float f32x2 __attribute__((ext_vector_type(2)));
typedef unsigned u32x4 __attribute__((ext_vector_type(4)));
typedef unsigned u32x2 __attribute__((ext_vector_type(2)));

#define DI __device__ __forceinline__
#define LAS __attribute__((address_space(3)))
#define MFMA(a, b, c) __builtin_amdgcn_mfma_f32_32x32x16_bf16((a), (b), (c), 0, 0, 0)
#define WAIT_V(n) asm volatile("s_waitcnt vmcnt(" #n ")" ::: "memory")
#define WAIT_L(n) asm volatile("s_waitcnt lgkmcnt(" #n ")" ::: "memory")
#define BAR() __builtin_amdgcn_s_barrier()
#define SCHED() __builtin_amdgcn_sched_barrier(0)

typedef LAS char* lptr;
#define PRIO(n) __builtin_amdgcn_s_setprio(n)

DI void glds16(const char* g, unsigned voff, lptr l) {
  unsigned sv;
  asm volatile("s_mov_b32 %0, m0\n\ts_mov_b32 m0, %3\n\ts_nop 0\n\tglobal_load_lds_dwordx4 %1, %2\n\ts_mov_b32 m0, %0"
               : "=&s"(sv) : "v"(voff), "s"(g), "s"((unsigned)(uintptr_t)l) : "memory");
}
DI void glds2(const char* g, unsigned voff, lptr l) {
  unsigned sv;
  asm volatile("s_mov_b32 %0, m0\n\ts_mov_b32 m0, %3\n\ts_nop 0\n\tglobal_load_lds_dwordx4 %1, %2\n\t"
               "global_load_lds_dwordx4 %1, %2 offset:1024\n\ts_mov_b32 m0, %0"
               : "=&s"(sv) : "v"(voff), "s"(g), "s"((unsigned)(uintptr_t)l) : "memory");
}
DI void glds4(const char* g, unsigned v0, unsigned v1, unsigned v2, unsigned v3, lptr l) {
  unsigned sv;
  asm volatile("s_mov_b32 %0, m0\n\ts_mov_b32 m0, %6\n\ts_nop 0\n\tglobal_load_lds_dwordx4 %1, %5\n\t"
               "global_load_lds_dwordx4 %2, %5 offset:1024\n\tglobal_load_lds_dwordx4 %3, %5 offset:2048\n\t"
               "global_load_lds_dwordx4 %4, %5 offset:3072\n\ts_mov_b32 m0, %0"
               : "=&s"(sv) : "v"(v0), "v"(v1), "v"(v2), "v"(v3), "s"(g), "s"((unsigned)(uintptr_t)l) : "memory");
}
DI void gtouch(const char* g, unsigned voff, lptr l) {
  unsigned sv;
  asm volatile("s_mov_b32 %0, m0\n\ts_mov_b32 m0, %3\n\ts_nop 0\n\tglobal_load_lds_dword %1, %2\n\ts_mov_b32 m0, %0"
               : "=&s"(sv) : "v"(voff), "s"(g), "s"((unsigned)(uintptr_t)l) : "memory");
}
DI void gtouch64(const char* p, lptr l) {
  unsigned sv;
  asm volatile("s_mov_b32 %0, m0\n\ts_mov_b32 m0, %2\n\ts_nop 0\n\tglobal_load_lds_dword %1, off\n\ts_mov_b32 m0, %0"
               : "=&s"(sv) : "v"(p), "s"((unsigned)(uintptr_t)l) : "memory");
}
#define CODE_RANGE(L, pc, voff, i)                                                                                     \
  const char* pc;                                                                                                      \
  unsigned voff;                                                                                                       \
  {                                                                                                                    \
    int l1, l2;                                                                                                        \
    asm volatile("s_getpc_b64 %0\n" L ":\n\ts_mov_b32 %1, .Lcode_end-" L "\n\ts_mov_b32 %2, .Lcode_end2-" L            \
                 : "=s"(pc), "=s"(l1), "=s"(l2));                                                                      \
    voff = (unsigned)max(min((i) * 128, max(l1, l2) - 4), 0);                                                          \
  }
typedef unsigned long long u64x4 __attribute__((ext_vector_type(4)));
#define KARG_LOAD(v) \
  u64x4 v;           \
  asm volatile("s_load_dwordx8 %0, %1, 0x0" : "=s"(v) : "s"(__builtin_amdgcn_kernarg_segment_ptr()))
DI char* gptr(unsigned long long a) { return (char*)(__attribute__((address_space(1))) char*)a; }
#define KARG_WAIT(v, a, b, ...) asm volatile("s_waitcnt lgkmcnt(0)" : "+s"(v), "+s"(a), "+s"(b) : __VA_ARGS__)
typedef __amdgpu_buffer_rsrc_t rsrc_t;
DI rsrc_t mkrsrc(const void* p, unsigned bytes) { return __builtin_amdgcn_make_buffer_rsrc((void*)p, (short)0, (int)bytes, 0x00020000); }
template <class V>
DI void st16(rsrc_t r, unsigned off, V v) { __builtin_amdgcn_raw_buffer_store_b128(__builtin_bit_cast(u32x4, v), r, (int)off, 0, 16); }
DI bf16x8 lds128(lptr p) { return *(LAS bf16x8*)p; }
DI s16x4 ldstr(lptr p) { return __builtin_amdgcn_ds_read_tr16_b64_v4i16((LAS s16x4*)p); }
DI bf16x8 cat(s16x4 a, s16x4 b) { return __builtin_shufflevector(a, b, 0, 1, 2, 3, 4, 5, 6, 7); }
DI unsigned cvtpk(float lo, float hi) {
  f32x2 v = {lo, hi};
  return __builtin_bit_cast(unsigned, __builtin_convertvector(v, bf16x2b));
}
DI bf16x8 pack8(const f32x16& x, int s) {
  u32x4 p;
#pragma unroll
  for (int i = 0; i < 4; ++i) p[i] = cvtpk(x[8 * s + 2 * i], x[8 * s + 2 * i + 1]);
  return __builtin_bit_cast(bf16x8, p);
}
DI float xhalf_max(float v) {
  u32x2 r = __builtin_amdgcn_permlane32_swap(__float_as_uint(v), __float_as_uint(v), false, false);
  return fmaxf(__uint_as_float(r[0]), __uint_as_float(r[1]));
}
DI float xhalf_min(float v) {
  u32x2 r = __builtin_amdgcn_permlane32_swap(__float_as_uint(v), __float_as_uint(v), false, false);
  return fminf(__uint_as_float(r[0]), __uint_as_float(r[1]));
}
DI float xhalf_add(float v) {
  u32x2 r = __builtin_amdgcn_permlane32_swap(__float_as_uint(v), __float_as_uint(v), false, false);
  return __uint_as_float(r[0]) + __uint_as_float(r[1]);
}
template <class Op>
DI float red4(const float (&v)[4], Op op) {
  const u32x2 a = __builtin_amdgcn_permlane32_swap(__float_as_uint(v[0]), __float_as_uint(v[1]), false, false);
  const u32x2 b = __builtin_amdgcn_permlane32_swap(__float_as_uint(v[2]), __float_as_uint(v[3]), false, false);
  const float x = op(__uint_as_float(a[0]), __uint_as_float(a[1])), y = op(__uint_as_float(b[0]), __uint_as_float(b[1]));
  const u32x2 c = __builtin_amdgcn_permlane16_swap(__float_as_uint(x), __float_as_uint(y), false, false);
  return op(__uint_as_float(c[0]), __uint_as_float(c[1]));
}
DI void bcast4(float r, float (&v)[4]) {
  const u32x2 c = __builtin_amdgcn_permlane16_swap(__float_as_uint(r), __float_as_uint(r), false, false);
  const u32x2 a = __builtin_amdgcn_permlane32_swap(c[0], c[0], false, false);
  const u32x2 b = __builtin_amdgcn_permlane32_swap(c[1], c[1], false, false);
  v[0] = __uint_as_float(a[0]); v[1] = __uint_as_float(a[1]); v[2] = __uint_as_float(b[0]); v[3] = __uint_as_float(b[1]);
}
DI f32x16 zero16() {
  f32x16 z;
#pragma unroll
  for (int i = 0; i < 16; ++i) z[i] = 0.f;
  return z;
}

template <int N, int I = 0, class Fn>
DI void static_for(Fn&& fn) {
  if constexpr (I < N) {
    fn(std::integral_constant<int, I>{});
    static_for<N, I + 1>(fn);
  }
}

constexpr float LOG2E = 1.4426950408889634f;
constexpr float QSCALE = 0.17677669529663687f * LOG2E;

constexpr size_t WS_QA = 0;
constexpr size_t WS_QB = 4u << 20;
constexpr size_t WS_O = 8u << 20;
constexpr size_t WS_KVA = 18u << 20;
constexpr unsigned WS_END = 32u << 20;
constexpr int KVT = 48, OSTR = 65536;

DI void dual_softmax(f32x16 p, f32x16& ep, f32x16& em) {
  float sp, sm;
  auto exps = [&](float mx, float mn) {
    f32x2 s0, s1;
#pragma unroll
    for (int i = 0; i < 16; i += 2) {
      const f32x2 x = {p[i], p[i + 1]}, a = x - f32x2{mx, mx}, b = x - f32x2{mn, mn};
      ep[i] = __builtin_amdgcn_exp2f(a[0]); ep[i + 1] = __builtin_amdgcn_exp2f(a[1]);
      em[i] = __builtin_amdgcn_exp2f(-b[0]); em[i + 1] = __builtin_amdgcn_exp2f(-b[1]);
      const f32x2 e0 = {ep[i], ep[i + 1]}, e1 = {em[i], em[i + 1]};
      s0 = i ? s0 + e0 : e0;
      s1 = i ? s1 + e1 : e1;
    }
    sp = xhalf_add(s0[0] + s0[1]);
    sm = xhalf_add(s1[0] + s1[1]);
  };
  p *= LOG2E;
  exps(0.f, 0.f);
  if (__builtin_amdgcn_ballot_w64(!(sp + sm < 0x1p60f)) != 0) {
    float mx = p[0], mn = p[0];
#pragma unroll
    for (int i = 1; i < 16; ++i) { mx = fmaxf(mx, p[i]); mn = fminf(mn, p[i]); }
    exps(xhalf_max(mx), xhalf_min(mn));
  }
  const float rp = __builtin_amdgcn_rcpf(sp), rm = __builtin_amdgcn_rcpf(sm);
#pragma unroll
  for (int i = 0; i < 16; ++i) { ep[i] *= rp; em[i] *= rm; }
}

constexpr int K1_XOFF = 0, K1_WOFF = 98304, K1_POFF = 159744, K1_LDS = 163840;
#define M16(a, b, c) __builtin_amdgcn_mfma_f32_16x16x32_bf16((a), (b), (c), 0, 0, 0)

struct Frags { bf16x8 x[4], wf[6]; };

__global__ __launch_bounds__(512, 2) void k1_qkv(const char* __restrict__, const char* __restrict__, const char* __restrict__,
                                                 const char* __restrict__, const char* __restrict__ wphi, char* __restrict__ ws) {
  KARG_LOAD(ka);
  __shared__ __attribute__((aligned(128))) char smem[K1_LDS];
  lptr lds = (lptr)smem;
  const int tid = threadIdx.x, lane = tid & 63;
  const int w = __builtin_amdgcn_readfirstlane(tid >> 6);
  const int bid = blockIdx.x, xcd = bid & 7, idx = bid >> 3;
  CODE_RANGE(".Lcode_k1", pc, voffC, tid)
  gtouch(pc, voffC, lds + K1_POFF);
  const int br = idx & 1, mt = (xcd >> 2) * 4 + ((idx >> 1) & 3), hp = (xcd & 3) * 4 + (idx >> 3);
  const int wm = w & 3, wn = w >> 2;

  const unsigned voffX = (unsigned)((lane >> 3) * 2048 + (((lane & 7) ^ (((w & 1) << 2) | (lane >> 4))) << 4));
  const unsigned voffW = (unsigned)((lane >> 3) * 6144 + (((lane & 7) ^ (((lane >> 4) & 1) << 2) ^ ((w & 1) << 1)) << 4));
  const bool lowv = w < 4;
  const unsigned voffM = lowv ? voffW : voffX;
  unsigned xo = mt * 524288 + w * 16384, wo = hp * 128 + (w & 3) * 8 * 6144 + (w >> 2) * 2048;
  KARG_WAIT(ka, xo, wo, "v"(voffX), "v"(voffW), "s"(w * 1024), "s"(br));
  const char *xA = gptr(ka[0]), *wA = gptr(ka[1]), *xB = gptr(ka[2]), *wB = gptr(ka[3]);
  const char* Xw = (br ? xB : xA) + xo;
  const char* Ww = (br ? wB : wA) + wo;
  const char* W = Ww - (w >> 2) * 2048;
  auto ldX = [&](int t, int slot, int piece_minus_w) {
    glds16(Xw + (size_t)(t * 128 + piece_minus_w * 16384), voffX, lds + K1_XOFF + slot * 32768 + (w + piece_minus_w) * 1024);
  };
  auto ldW = [&](int u, int slot) {
    glds16(Ww + (size_t)u * (32 * 6144), voffW, lds + K1_WOFF + slot * 12288 + w * 1024);
  };
  auto ldW8 = [&](int u, int slot) {
    glds16(W + (size_t)(u * 32 * 6144 + 4096), voffW, lds + K1_WOFF + slot * 12288 + (8 + (w & 3)) * 1024);
  };
  const char* Mp = lowv ? Ww + 4096 + 5 * (32 * 6144) : Xw + 3 * 128 + 12 * 16384;
  const unsigned Mstep = lowv ? 2 * 32 * 6144 : 128, Mdiff = lowv ? 32 * 6144 : 4 * 16384;
  const int Ml = w * 1024 + (lowv ? K1_WOFF + 8 * 1024 : K1_XOFF + 12 * 1024);

#pragma unroll
  for (int t = 0; t < 2; ++t) {
    ldX(t, t, 0); ldX(t, t, 8); ldX(t, t, 16); ldX(t, t, 24);
    ldW(2 * t, 2 * t); ldW(2 * t + 1, 2 * t + 1);
    ldW8(2 * t + (w >> 2), 2 * t + (w >> 2));
    if (t == 0) BAR();
  }
  const bool tch = br == 0 && w == 7;
  const int bi = (idx >> 1) * 8 + xcd, ox = mt * 524288 + hp * 32768;
  constexpr int R1 = 128, N1 = 12, R2 = R1 + N1 * 128 / 6, N2 = 48 - R2 * 6 / 128;
  if (tch) {
    const int gq = lane >> 4, c = lane & 15;
    const char* p = (gq & 2) ? ((gq & 1) ? wA : wB) + R1 * 6144 + bi * (N1 * 1024) + min(c, N1 - 1) * 1024 : ((gq & 1) ? xA : xB) + ox + c * 2048;
    gtouch64(p + 384, lds + K1_POFF);
  }

  const int c16 = lane & 15, g = lane >> 4;
  const int q4 = (lane >> 2) & 3, p4 = lane & 3;
  const unsigned xbase = (unsigned)(K1_XOFF + (wm * 64 + c16) * 128 + ((g ^ (c16 >> 1)) << 4));
  const unsigned wbase = (unsigned)(K1_WOFF + (8 * g + q4) * 128 + ((wn ^ (q4 >> 1)) << 6) + ((g & 1) << 5) + p4 * 8);
  f32x4 acc[6][4];
#pragma unroll
  for (int j = 0; j < 6; ++j)
#pragma unroll
    for (int i = 0; i < 4; ++i) acc[j][i] = f32x4{0.f, 0.f, 0.f, 0.f};
  auto rd1 = [&](Frags& n, int i, lptr xa, lptr wa, lptr wb) {
    if (i < 4) n.x[i] = lds128(xa + i * 2048);
    else {
      const int j = (i - 4) >> 1;
      lptr p = ((j & 1) ? wb : wa) + (j >> 1) * 4096 + (i & 1) * 512;
      const s16x4 v = ldstr(p);
      if (i & 1) { n.wf[j][4] = v[0]; n.wf[j][5] = v[1]; n.wf[j][6] = v[2]; n.wf[j][7] = v[3]; }
      else { n.wf[j][0] = v[0]; n.wf[j][1] = v[1]; n.wf[j][2] = v[2]; n.wf[j][3] = v[3]; }
    }
  };
  auto unit = [&](const Frags& c, Frags& n, auto RD, int xslot, int s, int wslot, auto hook) {
    lptr xa = lds + ((xbase + xslot * 32768) ^ (unsigned)(s << 6));
    lptr wa = lds + (wbase + wslot * 12288);
    lptr wb = lds + ((wbase + wslot * 12288) ^ 32u);
    static_for<24>([&](auto M) {
      constexpr int m = decltype(M)::value, j = m >> 2, i = m & 3;
      SCHED();
      if constexpr (j < 4) acc[j][i] = M16(c.wf[j], c.x[i], acc[j][i]);
      else acc[j][i] = M16(c.x[i], c.wf[j], acc[j][i]);
      SCHED();
      if constexpr (decltype(RD)::value && m < 16) rd1(n, m == 0 ? 0 : (m < 3 ? m + 3 : (m < 6 ? m - 2 : m)), xa, wa, wb);
      hook(M);
    });
    SCHED();
  };
  using T = std::integral_constant<bool, true>;
  using F = std::integral_constant<bool, false>;

  Frags f0, f1;
  if (tch) WAIT_V(8); else WAIT_V(7);
  BAR();
#pragma unroll
  for (int i = 0; i < 16; ++i) rd1(f0, i, lds + xbase, lds + wbase, lds + (wbase ^ 32u));
  int xs = 0, ws0 = 0;
  s16x4 pw[4];
  auto stage = [&](int t, auto LP, auto LC, auto WV, auto LAST) {
    const int xs1 = xs == 2 ? 0 : xs + 1, xsp = xs == 0 ? 2 : xs - 1;
    const int ws1 = ws0 == 4 ? 0 : ws0 + 1, ws2 = ws1 == 4 ? 0 : ws1 + 1, wsp = ws0 == 0 ? 4 : ws0 - 1;
    unit(f0, f1, T{}, xs, 1, ws1, [&](auto M) {
      constexpr int m = decltype(M)::value;
      if constexpr (m == 3) PRIO(1);
      if constexpr (m == 11) PRIO(0);
      if constexpr (decltype(LP)::value) {
        if constexpr (m == 3) glds16(Mp - Mstep + Mdiff, voffM, lds + Ml + (lowv ? wsp * 12288 : xsp * 32768 + 4096));
        if constexpr (m == 10) ldW(2 * t + 4, wsp);
      }
      if constexpr (decltype(WV)::value == 6) {
        if constexpr (m == 1 || m == 4 || m == 7 || m == 10) ldX(2, 2, (m / 3) * 8);
        if constexpr (m == 13) ldW(4, 4);
        if constexpr (m == 16) ldW8(4, 4);
      }
      if constexpr (m == 17 && decltype(WV)::value >= 0) {
        WAIT_L(0);
        if constexpr (decltype(WV)::value == 6) WAIT_V(6);
        if constexpr (decltype(WV)::value == 5) WAIT_V(5);
        if constexpr (decltype(WV)::value == 0) WAIT_V(0);
        BAR();
        PRIO(3);
      }
      if constexpr (decltype(LC)::value >= 1) {
        if constexpr (m == 18) ldW(2 * t + 5, ws0);
        if constexpr (m == 22) {
          if constexpr (decltype(LC)::value == 2) glds16(Mp, voffM, lds + Ml + (lowv ? ws0 * 12288 : xs * 32768));
          else ldW8(2 * t + 5, ws0);
        }
      }
    });
    unit(f1, f0, std::integral_constant<bool, !decltype(LAST)::value>{}, xs1, 0, ws2, [&](auto M) {
      constexpr int m = decltype(M)::value;
      if constexpr (m == 19) PRIO(2);
      if constexpr (decltype(LC)::value == 2) {
        if constexpr (m == 5) ldX(t + 3, xs, 0);
        if constexpr (m == 12) ldX(t + 3, xs, 8);
        if constexpr (m == 19) ldX(t + 3, xs, 24);
      }
      if constexpr (decltype(LAST)::value && m < 4) pw[m] = ldstr(lds + K1_POFF + wn * 2048 + (4 * g + q4) * 64 + p4 * 8 + (m & 1) * 1024 + (m >> 1) * 32);
    });
    xs = xs1;
    ws0 = ws2;
    Mp += Mstep;
  };
  using I5 = std::integral_constant<int, 5>;
  using I2 = std::integral_constant<int, 2>;
  using I1 = std::integral_constant<int, 1>;
  using I0 = std::integral_constant<int, 0>;
  using IN = std::integral_constant<int, -1>;
  stage(0, F{}, I2{}, std::integral_constant<int, 6>{}, F{});
  if (tch) {
    gtouch64(((lane & 32) ? wA : wB) + R2 * 6144 + bi * (N2 * 1024) + min(lane & 31, N2 - 1) * 1024 + 384, lds + K1_POFF);
    gtouch64(((lane & 16) ? xA : xB) + ox + (lane & 15) * 2048 + 1408, lds + K1_POFF);
  }
  glds16(wphi + hp * 4096 + (w & 3) * 1024, lane * 16, lds + K1_POFF + (w & 3) * 1024);
#pragma unroll 1
  for (int t = 1; t < 13; ++t) stage(t, T{}, I2{}, I5{}, F{});
  stage(13, T{}, I1{}, I5{}, F{});
  stage(14, F{}, I0{}, I0{}, F{});
  gtouch(pc, voffC, lds + K1_WOFF + 2 * 12288);
  stage(15, F{}, I0{}, IN{}, T{});

  const int head = hp * 2 + wn;
  const int tb = mt * 8 + wm * 2;
  auto pk = [](const f32x4& a, const f32x4& b, float sc) {
    return __builtin_bit_cast(bf16x8, u32x4{cvtpk(a[0] * sc, a[1] * sc), cvtpk(a[2] * sc, a[3] * sc), cvtpk(b[0] * sc, b[1] * sc), cvtpk(b[2] * sc, b[3] * sc)});
  };
  const rsrc_t rw = mkrsrc(ws, WS_END);
  const int fo = (g >> 1) * 1024 + (g & 1) * 512;
  if (br == 0) {
    const unsigned qd = WS_QA + ((head * 64 + tb) * 2048) + fo + c16 * 16;
    const unsigned kt = WS_KVA + ((head * 2 + (tb >> 5)) * KVT + (tb & 31)) * 4096 + fo;
    const unsigned kd = kt + c16 * 16, vd = kd + 2048;
#pragma unroll
    for (int i = 0; i < 4; ++i) {
      st16(rw, kd + (i >> 1) * 4096 + (i & 1) * 256, pk(acc[2][i], acc[3][i], 1.f));
      st16(rw, vd + (i >> 1) * 4096 + (i & 1) * 256, pk(acc[4 + (i & 1)][i & 2], acc[4 + (i & 1)][(i & 2) + 1], 1.f));
      st16(rw, qd + (i >> 1) * 2048 + (i & 1) * 256, pk(acc[0][i], acc[1][i], QSCALE));
    }
    return;
  }
  {
    const unsigned qd = WS_QB + ((head * 64 + tb) * 2048) + fo + c16 * 16;
#pragma unroll
    for (int i = 0; i < 4; ++i) st16(rw, qd + (i >> 1) * 2048 + (i & 1) * 256, pk(acc[0][i], acc[1][i], 1.f));
  }
  f32x4 ep[4][2], em[4][2];
  {
    const bf16x8 wf0 = cat(pw[0], pw[1]), wf1 = cat(pw[2], pw[3]);
    const f32x4 z4 = {0.f, 0.f, 0.f, 0.f};
    f32x4 p[4][2];
    float mx[4], mn[4], sp[4], sm[4], rp, rm;
#pragma unroll
    for (int i = 0; i < 4; ++i) {
      const bf16x8 kf = pk(acc[2][i], acc[3][i], 1.f);
      p[i][0] = M16(wf0, kf, z4);
      p[i][1] = M16(wf1, kf, z4);
    }
    auto exps = [&](auto SHIFT) {
#pragma unroll
      for (int i = 0; i < 4; ++i) {
        f32x2 s0, s1;
#pragma unroll
        for (int u = 0; u < 4; ++u) {
          const int ft = u >> 1, r = (u & 1) * 2;
          f32x2 a = {p[i][ft][r], p[i][ft][r + 1]}, b = a;
          if constexpr (decltype(SHIFT)::value) { a -= f32x2{mx[i], mx[i]}; b -= f32x2{mn[i], mn[i]}; }
          ep[i][ft][r] = __builtin_amdgcn_exp2f(a[0]); ep[i][ft][r + 1] = __builtin_amdgcn_exp2f(a[1]);
          em[i][ft][r] = __builtin_amdgcn_exp2f(-b[0]); em[i][ft][r + 1] = __builtin_amdgcn_exp2f(-b[1]);
          const f32x2 e0 = {ep[i][ft][r], ep[i][ft][r + 1]}, e1 = {em[i][ft][r], em[i][ft][r + 1]};
          s0 = u ? s0 + e0 : e0;
          s1 = u ? s1 + e1 : e1;
        }
        sp[i] = s0[0] + s0[1];
        sm[i] = s1[0] + s1[1];
      }
      rp = red4(sp, [](float x, float y) { return x + y; });
      rm = red4(sm, [](float x, float y) { return x + y; });
    };
#pragma unroll
    for (int i = 0; i < 4; ++i) { p[i][0] *= LOG2E; p[i][1] *= LOG2E; }
    exps(F{});
    if (__builtin_amdgcn_ballot_w64(!(rp + rm < 0x1p60f)) != 0) {
#pragma unroll
      for (int i = 0; i < 4; ++i) {
        mx[i] = fmaxf(p[i][0][0], p[i][1][0]);
        mn[i] = fminf(p[i][0][0], p[i][1][0]);
#pragma unroll
        for (int r = 1; r < 4; ++r) {
          mx[i] = fmaxf(fmaxf(mx[i], p[i][0][r]), p[i][1][r]);
          mn[i] = fminf(fminf(mn[i], p[i][0][r]), p[i][1][r]);
        }
      }
      bcast4(red4(mx, [](float x, float y) { return fmaxf(x, y); }), mx);
      bcast4(red4(mn, [](float x, float y) { return fminf(x, y); }), mn);
      exps(T{});
    }
    bcast4(__builtin_amdgcn_rcpf(rp), sp);
    bcast4(__builtin_amdgcn_rcpf(rm), sm);
    const unsigned pb = 32768 + w * 8192;
    const unsigned pwa = pb + c16 * 128 + ((g ^ (((c16 >> 1) & 1) << 2) ^ (c16 >> 2)) << 4);
    const unsigned pra = pb + (4 * g + q4) * 128 + ((p4 ^ ((q4 >> 1) << 2) ^ g) << 4);
    bf16x8 ph[2][4];
#pragma unroll
    for (int bk = 0; bk < 2; ++bk) {
#pragma unroll
      for (int i = 2 * bk; i < 2 * bk + 2; ++i) {
        *(LAS bf16x8*)(lds + pwa + i * 2048) = pk(ep[i][0], ep[i][1], sp[i]);
        *(LAS bf16x8*)(lds + (pwa ^ 64u) + i * 2048) = pk(em[i][0], em[i][1], sm[i]);
      }
      SCHED();
#pragma unroll
      for (int u = 0; u < 4; ++u) {
        lptr pr = lds + (pra ^ (unsigned)((u >> 1) * 64)) + bk * 4096 + (u & 1) * 8;
        ph[bk][u] = cat(ldstr(pr), ldstr(pr + 2048));
      }
      SCHED();
    }
    f32x4 y[4][2], z = z4;
#pragma unroll
    for (int u = 0; u < 4; ++u) { y[u][0] = z4; y[u][1] = z4; }
#pragma unroll
    for (int bk = 0; bk < 2; ++bk) {
      const bf16x8 v0 = pk(acc[4][2 * bk], acc[4][2 * bk + 1], 1.f), v1 = pk(acc[5][2 * bk], acc[5][2 * bk + 1], 1.f);
#pragma unroll
      for (int u = 0; u < 4; ++u) {
        y[u][0] = M16(ph[bk][u], v0, y[u][0]);
        y[u][1] = M16(ph[bk][u], v1, y[u][1]);
      }
    }
    SCHED();
#pragma unroll
    for (int u = 0; u < 8; ++u) {
      const unsigned one = c16 == (u & 3) ? 0x3f803f80u : 0u;
      z = M16(ph[u >> 2][u & 3], __builtin_bit_cast(bf16x8, u32x4{one, one, one, one}), z);
      *(LAS f32x4*)(lds + pb + u * 1024 + lane * 16) = y[u >> 1][u & 1];
    }
    const unsigned dst = WS_KVA + head * (2 * KVT * 4096);
    WAIT_L(0);
    BAR();
    lptr pr = lds + 32768 + wn * 32768 + wm * 2048 + lane * 16;
    f32x4 t[8];
#pragma unroll
    for (int u = 0; u < 8; ++u) t[u] = *(LAS f32x4*)(pr + (u >> 1) * 8192 + (u & 1) * 1024);
    SCHED();
    if (c16 < 4)
      __builtin_amdgcn_raw_buffer_store_b64(u32x2{cvtpk(z[0], z[1]), cvtpk(z[2], z[3])}, rw,
                                            (int)(dst + 40 * 4096 + c16 * 1024 + (g & 1) * 512 + (mt * 4 + wm) * 16 + (g >> 1) * 8), 0, 0);
    const unsigned d = dst + ((wm >> 1) * KVT + 32 + mt) * 4096 + (wm & 1) * 2048 + lane * 16;
    st16(rw, d, (t[0] + t[2]) + (t[4] + t[6]));
    st16(rw, d + 1024, (t[1] + t[3]) + (t[5] + t[7]));
  }
}

constexpr int K2_CB = 0, K2_QB = 131072, K2_PH = K2_QB + 16384, K2_KVF = K2_PH + 2048, K2_ML = K2_KVF + 4096,
              K2_JUNK = K2_ML + 4096, K2_LDS = K2_JUNK + 256;
typedef float f32x8 __attribute__((ext_vector_type(8)));
DI bf16x16b half16(const f32x16& s, int hi) {
  f32x8 t = hi ? __builtin_shufflevector(s, s, 8, 9, 10, 11, 12, 13, 14, 15) : __builtin_shufflevector(s, s, 0, 1, 2, 3, 4, 5, 6, 7);
  return __builtin_bit_cast(bf16x16b, t);
}
#define SPV(v, s, hi, o) __builtin_amdgcn_smfmac_f32_32x32x32_bf16(__builtin_bit_cast(bf16x8b, v), half16(s, hi), o, 0xDDDD, 0, 0)
#define SUM(sel, s, hi, l) __builtin_amdgcn_smfmac_f32_16x16x64_bf16(sel, half16(s, hi), l, 0xDDDD, 0, 0)

__global__ __launch_bounds__(768, 3) void k2_attn(const char* __restrict__, const char* __restrict__, const char* __restrict__,
                                                  char* __restrict__) {
  KARG_LOAD(ka);
  __shared__ __attribute__((aligned(128))) char smem[K2_LDS];
  lptr lds = (lptr)smem;
  const int tid = threadIdx.x, lane = tid & 63;
  const int wid = __builtin_amdgcn_readfirstlane(tid >> 6), w = wid < 4 ? wid * 2 : wid - 4;
  const int bid = blockIdx.x, xcd = bid & 7, idx = bid >> 3;
  const int head = xcd * 4 + (idx >> 3), qb = idx & 7;
  const int qp = w & 3, kh = w >> 2;
  const int h = lane >> 5;
  const int q4 = (lane >> 2) & 3, p4 = lane & 3, blk = (lane >> 4) & 1;
  const unsigned voff = (unsigned)(lane * 16);
  const int lk = (lane & 35) | ((lane & 4) << 2) | ((lane >> 1) & 12), lt = qp < 2 ? lk : lane;
  const unsigned vofft = (unsigned)(lt * 16);
  const int tb = qb * 8 + w;
  unsigned qo = (head * 64 + tb) * 2048, ko = WS_KVA + (head * 2 + kh) * (KVT * 4096) + qp * 1024;
  CODE_RANGE(".Lcode_k2", pc, voffC, tid)
  KARG_WAIT(ka, qo, ko, "v"(voff), "v"(vofft), "s"(w * 2048 + 14 * 8192), "s"(w * 1024));
  const char *wphi = gptr(ka[0]), *woA = gptr(ka[1]), *woB = gptr(ka[2]);
  char* ws = gptr(ka[3]);
  const char* kvp = ws + ko;
  const char* kvg = kvp - qp * 1024;
  auto ldt = [&](int t) { glds16(kvp + t * 4096, vofft, lds + (t & 15) * 8192 + w * 1024); };

  const rsrc_t rw = mkrsrc(ws, WS_END);
  if (__builtin_expect(wid < 4, 0)) {
    auto ld2 = [&](int t0, int t1) {
#pragma unroll
      for (int t = t0; t < t1; ++t) glds2(kvp + t * 4096, vofft, lds + (t & 15) * 8192 + w * 1024);
    };
    const char* pfw = ((xcd >> 2) ? woB : woA) + (idx * 8 + w) * 8192;
    bf16x8 ph[4], zf[4];
    auto phi = [&](int i) {
      lptr pa = lds + K2_PH + (4 * h + q4) * 64 + blk * 32 + p4 * 8;
      lptr ql = lds + K2_QB + (w + i) * 2048 + lane * 16;
      f32x16 p = MFMA(cat(ldstr(pa), ldstr(pa + 1024)), lds128(ql), zero16()), ep, em;
      p = MFMA(cat(ldstr(pa + 512), ldstr(pa + 1536)), lds128(ql + 1024), p);
      dual_softmax(p, ep, em);
      ph[0] = pack8(ep, 0); ph[1] = pack8(ep, 1); ph[2] = pack8(em, 0); ph[3] = pack8(em, 1);
      asm volatile("" : "+v"(ph[0]), "+v"(ph[1]), "+v"(ph[2]), "+v"(ph[3]));
    };
    auto outb = [&](int i) {
      f32x16 num = zero16(), den = zero16();
#pragma unroll
      for (int u = 0; u < 4; ++u) {
        den = MFMA(zf[u], ph[u], den);
        num = MFMA(lds128(lds + K2_KVF + u * 1024 + lane * 16), ph[u], num);
      }
      f32x2 ds = {den[0], den[1]};
#pragma unroll
      for (int u = 2; u < 16; u += 2) ds += f32x2{den[u], den[u + 1]};
      const float rd = __builtin_amdgcn_rcpf(xhalf_add(ds[0] + ds[1]) + 1e-6f);
#pragma unroll
      for (int u = 0; u < 16; ++u) num[u] *= rd;
      const unsigned odb = WS_O + (64 + tb + i) * OSTR + head * 2048 + lane * 16;
      st16(rw, odb, pack8(num, 0));
      st16(rw, odb + 1024, pack8(num, 1));
    };
    PRIO(3);
    BAR();
    ld2(2, 6);
    BAR();
    ld2(6, 10);
    WAIT_V(8);
    BAR();
    ld2(10, 17);
    glds4(ws + WS_QB + qo, voff, voff, voff, voff, lds + K2_QB + w * 2048);
    glds16(wphi + head * 2048 + (wid & 1) * 1024, voff, lds + K2_PH + (wid & 1) * 1024);
    WAIT_V(19);
    BAR();
    ld2(17, 21);
    WAIT_V(8);
    f32x4 pp[2][8];
#pragma unroll
    for (int i = 0; i < 16; ++i) pp[i & 1][i >> 1] = *(const f32x4*)(kvp + (32 + (i >> 1)) * 4096 + (i & 1) * 1024 + lane * 16);
#pragma unroll
    for (int i = 0; i < 4; ++i) zf[i] = *(const bf16x8*)(ws + WS_KVA + (head * 2 * KVT + 40) * 4096 + i * 1024 + lane * 16);
    SCHED();
    BAR();
    SCHED();
#pragma unroll
    for (int i = 0; i < 2; ++i) {
      const f32x4 a = ((pp[i][0] + pp[i][1]) + (pp[i][2] + pp[i][3])) + ((pp[i][4] + pp[i][5]) + (pp[i][6] + pp[i][7]));
      *(LAS u32x2*)(lds + K2_KVF + wid * 1024 + (lane & 16) * 32 + i * 256 + (lane & 15) * 16 + h * 8) = u32x2{cvtpk(a[0], a[1]), cvtpk(a[2], a[3])};
    }
    asm volatile("" : "+v"(zf[0]), "+v"(zf[1]), "+v"(zf[2]), "+v"(zf[3]));
    ld2(21, 25);
    WAIT_V(8);
    phi(0);
    WAIT_L(0);
    BAR();
    ld2(25, 29);
    WAIT_V(8);
    outb(0);
    phi(1);
    BAR();
    ld2(29, 32);
    WAIT_V(0);
    BAR();
    gtouch(pfw, lane * 128, lds + K2_JUNK);
    gtouch(pfw + 8192, lane * 128, lds + K2_JUNK);
    gtouch(pc, voffC, lds + K2_JUNK);
    outb(1);
    WAIT_V(0);
    return;
  }
  glds2(ws + WS_QA + qo, voff, lds + 14 * 8192 + w * 2048);
  ldt(0); ldt(1);
  SCHED();
  BAR();
  bf16x8b sel;
  {
    const bool on = (lane & 3) == 0;
    const bool odd = (lane >> 2) & 1;
#pragma unroll
    for (int j = 0; j < 8; ++j) sel[j] = (on && ((j >= 4) == odd)) ? (__bf16)1.0f : (__bf16)0.0f;
  }
  float z0 = 0.f;
  asm volatile("" : "+v"(z0));
  f32x16 oa = z0, ob = z0, sa, sb;
  f32x4 la = z0, lb = z0;
  float ma = 0.f, mb = 0.f;
  lptr kb = lds + kh * 4096 + lane * 16;
  lptr ql0 = lds + 14 * 8192 + qp * 4096 + lane * 16;
#define PIN() asm volatile("" : "+v"(oa), "+v"(ob), "+v"(la), "+v"(lb))
  PIN();
  asm volatile("" : "+v"(sel), "+v"(kb), "+v"(ql0));
  WAIT_V(0);
  BAR();
  SCHED();
  const bf16x8 qa0 = lds128(ql0 + (kh ^ 1) * 2048);
  bf16x8 k0 = lds128(kb);
  const bf16x8 qa1 = lds128(ql0 + (kh ^ 1) * 2048 + 1024);
  bf16x8 k1 = lds128(kb + 1024);
  const bf16x8 qb0 = lds128(ql0 + kh * 2048), qb1 = lds128(ql0 + kh * 2048 + 1024);
  bf16x8 v0 = lds128(kb + 2048), v1;
  SCHED();
  sa = MFMA(k0, qa0, zero16());
  sa = MFMA(k1, qa1, sa);
#define EXP(s, i) s[i] = __builtin_amdgcn_exp2f(s[i])
  auto step = [&](lptr cur, lptr nxt, auto LAST, auto FIRST, bool low = false) {
    SCHED();
    if constexpr (!decltype(FIRST)::value) ob = SPV(v1, sb, 1, ob);
    SCHED();
    v1 = lds128(cur + 3072);
    EXP(sa, 0); EXP(sa, 1); EXP(sa, 2);
    SCHED();
    if constexpr (!decltype(FIRST)::value) lb = SUM(sel, sb, 1, lb);
    SCHED();
    EXP(sa, 3); EXP(sa, 4);
    SCHED();
    sb = MFMA(k0, qb0, zero16());
    SCHED();
    if constexpr (!decltype(LAST)::value) k0 = lds128(nxt);
    EXP(sa, 5); EXP(sa, 6); EXP(sa, 7);
    SCHED();
    sb = MFMA(k1, qb1, sb);
    SCHED();
    if constexpr (!decltype(LAST)::value) k1 = lds128(nxt + 1024);
    EXP(sa, 8); EXP(sa, 9); EXP(sa, 10);
    SCHED();
    oa = SPV(v0, sa, 0, oa);
    SCHED();
    EXP(sa, 11); EXP(sa, 12); EXP(sa, 13);
    SCHED();
    la = SUM(sel, sa, 0, la);
    SCHED();
    EXP(sa, 14); EXP(sa, 15);
    SCHED();
    if (low) PRIO(0);
    EXP(sb, 0);
    SCHED();
    oa = SPV(v1, sa, 1, oa);
    SCHED();
    EXP(sb, 1); EXP(sb, 2);
    SCHED();
    la = SUM(sel, sa, 1, la);
    SCHED();
    EXP(sb, 3); EXP(sb, 4);
    SCHED();
    if constexpr (!decltype(LAST)::value) sa = MFMA(k0, qa0, zero16());
    SCHED();
    EXP(sb, 5); EXP(sb, 6); EXP(sb, 7);
    SCHED();
    if constexpr (!decltype(LAST)::value) sa = MFMA(k1, qa1, sa);
    SCHED();
    EXP(sb, 8); EXP(sb, 9); EXP(sb, 10);
    SCHED();
    ob = SPV(v0, sb, 0, ob);
    SCHED();
    if constexpr (!decltype(LAST)::value) v0 = lds128(nxt + 2048);
    EXP(sb, 11); EXP(sb, 12); EXP(sb, 13);
    SCHED();
    lb = SUM(sel, sb, 0, lb);
    SCHED();
    EXP(sb, 14); EXP(sb, 15);
    SCHED();
  };
  using T = std::integral_constant<bool, true>;
  using F = std::integral_constant<bool, false>;
  step(kb, kb + 8192, F{}, T{});
  lptr gb = kb, gn = kb + 32768;
#pragma unroll 1
  for (int j = 0; j < 7; ++j) {
    if (j < 6) BAR();
    PRIO(2);
    step(gb + 8192, gb + 16384, F{}, F{});
    step(gb + 16384, gb + 24576, F{}, F{});
    step(gb + 24576, gn, F{}, F{});
    PRIO(1);
    step(gn, gn + 8192, F{}, F{}, true);
    gb = gn;
    gn = kb + ((j + 2) & 3) * 32768;
  }
  PRIO(1);
  step(gb + 8192, gb + 16384, F{}, F{});
  step(gb + 16384, gb + 24576, F{}, F{});
  PRIO(0);
  step(gb + 24576, gb, T{}, F{});
  ob = SPV(v1, sb, 1, ob);
  lb = SUM(sel, sb, 1, lb);
  PIN();
  {
    f32x2 c = {0.f, 0.f}, d = c;
    const f32x2 z2 = {0.f, 0.f};
#pragma unroll
    for (int i = 0; i < 16; i += 2) {
      c = __builtin_elementwise_fma(f32x2{oa[i], oa[i + 1]}, z2, c);
      d = __builtin_elementwise_fma(f32x2{ob[i], ob[i + 1]}, z2, d);
    }
    c += d;
    const bool bad = !(c[0] + c[1] + la[0] + lb[0] < 0x1p80f) || !(fminf(la[0], lb[0]) > 0x1p-24f);
    if (__builtin_amdgcn_ballot_w64(bad) != 0) {
      oa = zero16(); ob = zero16();
      ma = -INFINITY; mb = -INFINITY;
      la = f32x4{0.f, 0.f, 0.f, 0.f}; lb = la;
#pragma unroll 1
      for (int t = 0; t < 32; ++t) {
        const char* g = kvg + (size_t)t * 4096 + lane * 16;
        const char* gk = kvg + (size_t)t * 4096 + lk * 16;
        k0 = *(const bf16x8*)gk; k1 = *(const bf16x8*)(gk + 1024); v0 = *(const bf16x8*)(g + 2048); v1 = *(const bf16x8*)(g + 3072);
        auto upd = [&](const bf16x8& q0, const bf16x8& q1, f32x16& o, f32x4& l, float& m) {
          f32x16 s = MFMA(k0, q0, zero16());
          s = MFMA(k1, q1, s);
          float tm = s[0];
#pragma unroll
          for (int i = 1; i < 16; ++i) tm = fmaxf(tm, s[i]);
          const float mn = fmaxf(m, xhalf_max(tm));
          const float al = __builtin_amdgcn_exp2f(m - mn);
          m = mn;
          l[0] *= al;
#pragma unroll
          for (int i = 0; i < 16; ++i) { o[i] *= al; s[i] = __builtin_amdgcn_exp2f(s[i] - mn); }
          o = SPV(v0, s, 0, o);
          o = SPV(v1, s, 1, o);
          l = SUM(sel, s, 0, l);
          l = SUM(sel, s, 1, l);
        };
        upd(qa0, qa1, oa, la, ma);
        upd(qb0, qb1, ob, lb, mb);
      }
    }
  }
  {
    lptr cw = lds + K2_CB + w * 4096 + lane * 16;
#pragma unroll
    for (int g = 0; g < 4; ++g) *(LAS f32x4*)(cw + g * 1024) = f32x4{oa[4 * g], oa[4 * g + 1], oa[4 * g + 2], oa[4 * g + 3]};
    *(LAS f32x2*)(lds + K2_ML + w * 512 + lane * 8) = f32x2{ma, la[0]};
  }
  WAIT_L(0);
  BAR();
  {
    lptr cr = lds + K2_CB + (w ^ 4) * 4096 + lane * 16;
    const f32x2 ml = *(LAS f32x2*)(lds + K2_ML + (w ^ 4) * 512 + lane * 8);
    f32x4 t[4];
#pragma unroll
    for (int g = 0; g < 4; ++g) t[g] = *(LAS f32x4*)(cr + g * 1024);
    const float mm = fmaxf(mb, ml[0]);
    const float a0 = __builtin_amdgcn_exp2f(mb - mm), a1 = __builtin_amdgcn_exp2f(ml[0] - mm);
    const float rl = __builtin_amdgcn_rcpf(lb[0] * a0 + ml[1] * a1);
    const float c0 = a0 * rl, c1 = a1 * rl;
#pragma unroll
    for (int i = 0; i < 16; ++i) ob[i] = ob[i] * c0 + t[i >> 2][i & 3] * c1;
    const unsigned oda = WS_O + (qb * 8 + qp * 2 + kh) * OSTR + head * 2048 + lane * 16;
    st16(rw, oda, pack8(ob, 0));
    st16(rw, oda + 1024, pack8(ob, 1));
  }
}

constexpr int K3_LDS = 163840;
struct Frags3 { bf16x8 o[4], w[4]; };

__global__ __launch_bounds__(768, 3) void k3_proj(const char* __restrict__, const char* __restrict__, const char* __restrict__,
                                                  char* __restrict__) {
  KARG_LOAD(ka);
  __shared__ __attribute__((aligned(128))) char smem[K3_LDS];
  lptr lds = (lptr)smem;
  const int tid = threadIdx.x, lane = tid & 63;
  const int wid = __builtin_amdgcn_readfirstlane(tid >> 6), w = wid < 4 ? wid * 2 : wid - 4;
  const int bid = blockIdx.x, xcd = bid & 7, idx = bid >> 3;
  const int br = xcd >> 2, mt = (xcd & 3) * 4 + (idx & 3), nt = idx >> 2;
  const int wm = w & 1, wn = (w >> 1) & 1, kh = w >> 2;
  const int c16 = lane & 15, g = lane >> 4, q4 = (lane >> 2) & 3, p4 = lane & 3;
  const unsigned voffO = (unsigned)(lane * 16);
  const unsigned voffW = (unsigned)(g * 2048 + ((c16 ^ (g << 1)) << 4)), voffW2 = voffW ^ 128u;
  unsigned oo = WS_O + (br * 64 + mt * 4 + (w >> 1)) * OSTR + (w & 1) * 2048, wo = nt * 256 + w * 8 * 2048;
  CODE_RANGE(".Lcode_k3", pc, voffC, lane)
  KARG_WAIT(ka, oo, wo, "v"(voffO), "v"(voffW), "v"(voffW2 + 7168), "v"(voffW + 14336), "v"(voffW2 + 21504), "s"(w * 2048), "s"(br));
  const char* ws = gptr(ka[2]);
  char* out = gptr(ka[3]);
  const char* og = ws + oo;
  const char* wg = gptr(br ? ka[1] : ka[0]) + wo;
  if (__builtin_expect(wid < 4, 1)) {
    int sl = 0;
#pragma unroll 1
    for (int t = 0; t < 16; ++t) {
      lptr l = lds + sl * 32768 + w * 2048;
      glds4(og + t * 4096, voffO, voffO, voffO, voffO, l);
      if (t >= 2) { WAIT_V(12); BAR(); }
      glds4(wg + (size_t)t * (64 * 2048), voffW, voffW2 + 7168, voffW + 14336, voffW2 + 21504, l + 16384);
      sl = sl == 4 ? 0 : sl + 1;
    }
    WAIT_V(8);
    BAR();
    WAIT_V(0);
    BAR();
    asm volatile(".Lcode_end2:\n\ts_endpgm" ::: "memory");
  }
  unsigned junk = 0;
  if (wid == 4) asm volatile("global_load_dword %0, %1, %2" : "=&v"(junk) : "v"(voffC), "s"(pc) : "memory");
  const unsigned obase = (unsigned)((wm * 2 + kh) * 4096 + kh * 2048 + g * 512 + c16 * 16);
  const int wrow = (g >> 1) * 16 + (g & 1) * 4 + q4;
  const unsigned wbase = (unsigned)(16384 + (kh * 32 + wrow) * 256 + (((wn * 4) ^ (wrow & 7)) << 5) + p4 * 8);
  auto rd1 = [&](Frags3& n, int i, unsigned ob, unsigned wb) {
    if (i < 4) n.o[i] = lds128(lds + ((ob ^ ((i >> 1) << 12)) + (i & 1) * 256));
    else {
      const int j = (i - 4) >> 1;
      const s16x4 v = ldstr(lds + ((wb ^ (j << 5)) + (i & 1) * 2048));
      if (i & 1) { n.w[j][4] = v[0]; n.w[j][5] = v[1]; n.w[j][6] = v[2]; n.w[j][7] = v[3]; }
      else { n.w[j][0] = v[0]; n.w[j][1] = v[1]; n.w[j][2] = v[2]; n.w[j][3] = v[3]; }
    }
  };
  f32x4 acc[4][4];
#pragma unroll
  for (int i = 0; i < 4; ++i)
#pragma unroll
    for (int j = 0; j < 4; ++j) acc[i][j] = f32x4{0.f, 0.f, 0.f, 0.f};
  Frags3 f0, f1;
  BAR();
#pragma unroll
  for (int i = 0; i < 12; ++i) rd1(f0, i, obase, wbase);
  int sl = 0;
  lptr xw = lds + 32768 + w * 8192 + lane * 16;
  auto stage = [&](const Frags3& c, Frags3& n, auto LAST) {
    constexpr bool last = decltype(LAST)::value;
    const int sl1 = sl == 4 ? 0 : sl + 1;
    WAIT_L(0);
    if constexpr (!last) BAR();
    const unsigned ob = obase + sl1 * 32768, wb = wbase + sl1 * 32768;
    static_for<16>([&](auto M) {
      constexpr int m = decltype(M)::value, i = last ? (m >> 2) ^ 2 : m & 3, j = last ? m & 3 : m >> 2;
      SCHED();
      acc[i][j] = M16(c.w[j], c.o[i], acc[i][j]);
      SCHED();
      if constexpr (!last && m < 12) rd1(n, m == 0 ? 0 : (m < 3 ? m + 3 : (m < 6 ? m - 2 : m)), ob, wb);
      if constexpr (last && m >= 4 && m < 12) *(LAS f32x4*)(xw + (m - 4) * 1024) = acc[2 + ((m - 4) >> 2)][m & 3];
    });
    SCHED();
    sl = sl1;
  };
  using T = std::integral_constant<bool, true>;
  using F = std::integral_constant<bool, false>;
#pragma unroll 1
  for (int t = 0; t < 14; t += 2) {
    stage(f0, f1, F{});
    stage(f1, f0, F{});
  }
  stage(f0, f1, F{});
  stage(f1, f0, T{});

  {
    WAIT_V(0);
    asm volatile("" ::"v"(junk));
    WAIT_L(0);
    BAR();
    lptr xr = lds + 32768 + (w ^ 4) * 8192 + lane * 16;
    const rsrc_t ro = mkrsrc(out, 8u << 20);
    const unsigned od = (mt * 128 + (wm * 2 + kh) * 32 + (c16 & 7)) * 4096 + br * 2048 + nt * 256 + wn * 128 + (c16 >> 3) * 64 + (g & 1) * 32 + (g >> 1) * 16;
    f32x4 x[8];
#pragma unroll
    for (int i = 0; i < 8; ++i) x[i] = *(LAS f32x4*)(xr + i * 1024);
#pragma unroll
    for (int i = 0; i < 2; ++i) {
      u32x4 o[2], o0, o1;
#pragma unroll
      for (int p = 0; p < 2; ++p) {
        const f32x4 a = acc[i][2 * p] + x[i * 4 + 2 * p], b = acc[i][2 * p + 1] + x[i * 4 + 2 * p + 1];
        const u32x2 x0 = __builtin_amdgcn_permlane16_swap(cvtpk(a[0], a[1]), cvtpk(b[0], b[1]), false, false);
        const u32x2 x1 = __builtin_amdgcn_permlane16_swap(cvtpk(a[2], a[3]), cvtpk(b[2], b[3]), false, false);
        o[p] = u32x4{x0[0], x1[0], x0[1], x1[1]};
      }
#pragma unroll
      for (int e = 0; e < 4; ++e) {
        o0[e] = __builtin_amdgcn_update_dpp(o[0][e], o[1][e], 0x128, 0xf, 0xc, false);
        o1[e] = __builtin_amdgcn_update_dpp(o[1][e], o[0][e], 0x128, 0xf, 0x3, false);
      }
      st16(ro, od + i * 16 * 4096, o0);
      st16(ro, od + i * 16 * 4096 + 8 * 4096, o1);
    }
  }
  asm volatile(".Lcode_end:");
}

extern "C" void kernel_launch(void* const* d_in, const int* in_sizes, int n_in, void* d_out, int out_size, void* d_ws,
                              size_t ws_size, hipStream_t stream) {
  const char* xA = (const char*)d_in[0];
  const char* wA = (const char*)d_in[1];
  const char* woA = (const char*)d_in[2];
  const char* xB = (const char*)d_in[3];
  const char* wB = (const char*)d_in[4];
  const char* wphi = (const char*)d_in[5];
  const char* woB = (const char*)d_in[6];
  char* ws = (char*)d_ws;
  k1_qkv<<<256, 512, 0, stream>>>(xA, wA, xB, wB, wphi, ws);
  k2_attn<<<256, 768, 0, stream>>>(wphi, woA, woB, ws);
  k3_proj<<<256, 768, 0, stream>>>(woA, woB, ws, (char*)d_out);
}
```
